```python
import torch

def _softmax(x, axis=-1):
    return torch.softmax(x, dim=axis)
def _gelu(x):
    return torch.nn.functional.gelu(x, approximate="tanh")
def _silu(x):
    return torch.nn.functional.silu(x)
def _sigmoid(x):
    return torch.sigmoid(x)
def _layernorm(x, axis=-1, eps=1e-5):
    m = x.mean(dim=axis, keepdim=True)
    v = x.var(dim=axis, keepdim=True, unbiased=False)
    return (x - m) / torch.sqrt(v + eps)
def _scatter_add(tgt, idx, src, axis=0):
    return tgt.clone().index_add_(axis, idx, src)
def _segment_sum(src, seg, n):
    out = torch.zeros((n,) + src.shape[1:], dtype=src.dtype)
    return out.index_add_(0, seg, src)
def _pad(x, pw):
    flat = tuple(v for pair in reversed(pw) for v in pair)
    return torch.nn.functional.pad(x, flat)

kernel_name = 'recurrent_scan__sliding_local_dilated__mixer_s_mlp__prompt'

def setup_inputs():

    _w = (lambda a: (lambda t: t.to(torch.bfloat16) if t.is_floating_point() else t)(torch.as_tensor(a)))
    def _A_setup():
        B, S, D = 1, 2048, 640
        return {
            "x": _w((lambda *s: torch.randn(*s, dtype=torch.bfloat16) * 0.05)(B, S, D)),
            "wqkv": _w((lambda *s: torch.randn(*s, dtype=torch.bfloat16) * 0.05)(D, 3 * D)),
            "wo": _w((lambda *s: torch.randn(*s, dtype=torch.bfloat16) * 0.05)(D, D)),
        }
    _A_in = _A_setup()

    def _B_setup():
        B, S, D, F = 1, 2048, 640, 1728
        return {
            "x":  _w((lambda *s: torch.randn(*s, dtype=torch.bfloat16) * 0.05)(B, S, D)),
            "w1": _w((lambda *s: torch.randn(*s, dtype=torch.bfloat16) * 3.2)(D, D) * (D ** -0.5)),
            "w2": _w((lambda *s: torch.randn(*s, dtype=torch.bfloat16) * 3.2)(D, D) * (D ** -0.5)),
            "w3": _w((lambda *s: torch.randn(*s, dtype=torch.bfloat16) * 3.2)(D, F) * (D ** -0.5)),
            "w4": _w((lambda *s: torch.randn(*s, dtype=torch.bfloat16) * 3.2)(F, D) * (F ** -0.5)),
        }
    _B_in = _B_setup()

    return {
        "A_x": _A_in["x"],
        "A_wqkv": _A_in["wqkv"],
        "A_wo": _A_in["wo"],
        "B_x": _B_in["x"],
        "B_w1": _B_in["w1"],
        "B_w2": _B_in["w2"],
        "B_w3": _B_in["w3"],
        "B_w4": _B_in["w4"],
    }

def reference(A_x, A_wqkv, A_wo, B_x, B_w1, B_w2, B_w3, B_w4):
    def _A_ref(x, wqkv, wo):
        B, S, D = x.shape
        H = 32; Dh = D // H
        W, dil = 8, 2
        qkv = (lambda x, *d: x.permute(*d))((x @ wqkv).reshape(B, S, 3, H, Dh), 2, 0, 3, 1, 4)
        q, k, v = qkv[0], qkv[1], qkv[2]
        scale = Dh ** -0.5
        pos = torch.arange(S)
        off = torch.arange(W) * dil
        idx = pos[:, None] - off[None, :]
        mask = idx >= 0
        idx_c = torch.where(mask, idx, 0).reshape(-1)
        kg = k[:, :, idx_c, :].reshape(B, H, S, W, Dh)
        vg = v[:, :, idx_c, :].reshape(B, H, S, W, Dh)
        logits = torch.einsum("bhsd,bhswd->bhsw", q, kg) * scale
        logits = torch.where(mask[None, None, :, :], logits, -1e9)
        attn = _softmax(logits, axis=-1)
        o = torch.einsum("bhsw,bhswd->bhsd", attn, vg)
        return (lambda x, *d: x.permute(*d))(o, 0, 2, 1, 3).reshape(B, S, D) @ wo

    def _B_ref(x, w1, w2, w3, w4):
        B, S, D = x.shape
        h = _gelu(_layernorm(x, axis=-1) @ w1)
        c = D // 4
        g0 = _pad(h[:, :, 0*c:1*c], ((0, 0), (1, 0), (0, 0)))[:, :S, :]
        g1 = _pad(h[:, :, 1*c:2*c], ((0, 0), (0, 1), (0, 0)))[:, 1:, :]
        g2 = _pad(h[:, :, 2*c:3*c], ((0, 0), (2, 0), (0, 0)))[:, :S, :]
        g3 = _pad(h[:, :, 3*c:   ], ((0, 0), (0, 2), (0, 0)))[:, 2:, :]
        hs = torch.concatenate([g0, g1, g2, g3], axis=-1) @ w2
        x1 = x + hs
        h2 = _gelu(_layernorm(x1, axis=-1) @ w3) @ w4
        return x1 + h2

    x = A_x
    xn = _layernorm(x, axis=-1)
    s = _A_ref(xn, A_wqkv, A_wo)
    g = _silu(_B_ref(xn, B_w1, B_w2, B_w3, B_w4))
    return x + s * g
```

```cpp
#include <hip/hip_runtime.h>
#include <hip/hip_bf16.h>
#include <stdint.h>

typedef unsigned short u16;
typedef short bf16x8 __attribute__((ext_vector_type(8)));
typedef float f32x4 __attribute__((ext_vector_type(4)));

constexpr int S = 2048, D = 640, F = 1728, H = 32, DH = 20;

__device__ __forceinline__ float bf2f(u16 b) { return __builtin_bit_cast(float, (unsigned)b << 16); }
__device__ __forceinline__ u16 f2bf(float f) { __bf16 h = (__bf16)f; return __builtin_bit_cast(u16, h); }
__device__ __forceinline__ float rb(float f) { return bf2f(f2bf(f)); }
__device__ __forceinline__ float gelu(float x) {
  float u = 0.7978845608028654f * (x + 0.044715f * x * x * x);
  return x / (1.0f + __expf(-2.0f * u));
}

__device__ __forceinline__ void ln_row(const float (&v)[10], float (&o)[10]) {
  float s = 0; for (int i = 0; i < 10; ++i) s += v[i];
  for (int m = 32; m >= 1; m >>= 1) s += __shfl_xor(s, m);
  float mean = s * (1.0f / D);
  float q = 0; for (int i = 0; i < 10; ++i) { float d = v[i] - mean; q += d * d; }
  for (int m = 32; m >= 1; m >>= 1) q += __shfl_xor(q, m);
  float r = 1.0f / sqrtf(q * (1.0f / D) + 1e-5f);
  for (int i = 0; i < 10; ++i) o[i] = rb((v[i] - mean) * r);
}
__global__ void ln_kernel(const u16* x, u16* xn, u16* xnn) {
  int row = blockIdx.x * 4 + (threadIdx.x >> 6), lane = threadIdx.x & 63;
  float v[10], o[10], o2[10];
  for (int i = 0; i < 10; ++i) v[i] = bf2f(x[row * D + lane + 64 * i]);
  ln_row(v, o);
  for (int i = 0; i < 10; ++i) xn[row * D + lane + 64 * i] = f2bf(o[i]);
  if (xnn) { ln_row(o, o2); for (int i = 0; i < 10; ++i) xnn[row * D + lane + 64 * i] = f2bf(o2[i]); }
}

template <int MODE>
__global__ void gemm_kernel(const u16* A, const u16* B, u16* C, int N, int K, const u16* r0, const u16* r1, const u16* r2) {
  __shared__ u16 As[64][40];
  __shared__ u16 Bs[64][40];
  int tid = threadIdx.x, lane = tid & 63, w = tid >> 6;
  int m0 = blockIdx.y * 64, n0 = blockIdx.x * 64;
  int wr = w >> 1, wc = w & 1;
  f32x4 acc[2][2] = {};
  for (int k0 = 0; k0 < K; k0 += 32) {
    { int r = tid >> 2, c = (tid & 3) * 8;
      int row = m0 + r; bool ok = true;
      if (MODE == 2) { int g = (k0 + c) / 160; int sh = g == 0 ? -1 : g == 1 ? 1 : g == 2 ? -2 : 2; row += sh; ok = row >= 0 && row < S; }
      uint4 v = ok ? *(const uint4*)(A + (size_t)row * K + k0 + c) : make_uint4(0, 0, 0, 0);
      *(uint4*)&As[r][c] = v; }
    { int k = tid >> 3, c = (tid & 7) * 8;
      uint4 v = *(const uint4*)(B + (size_t)(k0 + k) * N + n0 + c);
      const u16* p = (const u16*)&v;
      for (int j = 0; j < 8; ++j) Bs[c + j][k] = p[j]; }
    __syncthreads();
    bf16x8 a[2], b[2];
    for (int i = 0; i < 2; ++i) a[i] = *(const bf16x8*)&As[wr * 32 + i * 16 + (lane & 15)][(lane >> 4) * 8];
    for (int i = 0; i < 2; ++i) b[i] = *(const bf16x8*)&Bs[wc * 32 + i * 16 + (lane & 15)][(lane >> 4) * 8];
    for (int i = 0; i < 2; ++i) for (int j = 0; j < 2; ++j) acc[i][j] = __builtin_amdgcn_mfma_f32_16x16x32_bf16(a[i], b[j], acc[i][j], 0, 0, 0);
    __syncthreads();
  }
  for (int i = 0; i < 2; ++i) for (int j = 0; j < 2; ++j) for (int e = 0; e < 4; ++e) {
    int row = m0 + wr * 32 + i * 16 + (lane >> 4) * 4 + e, col = n0 + wc * 32 + j * 16 + (lane & 15);
    float v = rb(acc[i][j][e]);
    size_t idx = (size_t)row * N + col;
    if (MODE == 1) v = gelu(v);
    if (MODE == 2) v = bf2f(r0[idx]) + v;
    if (MODE == 3) { float b = rb(bf2f(r0[idx]) + v); float g = rb(b / (1.0f + __expf(-b))); v = bf2f(r2[idx]) + rb(bf2f(r1[idx]) * g); }
    C[idx] = f2bf(v);
  }
}

__global__ void attn_kernel(const u16* qkv, u16* o) {
  int idx = blockIdx.x * 256 + threadIdx.x; int s = idx / H, h = idx % H;
  float q[DH]; for (int d = 0; d < DH; ++d) q[d] = bf2f(qkv[(size_t)s * 3 * D + h * DH + d]);
  float l[8]; float mx = -3e38f;
  for (int w = 0; w < 8; ++w) { int p = s - 2 * w; float a = 0;
    if (p >= 0) { for (int d = 0; d < DH; ++d) a += q[d] * bf2f(qkv[(size_t)p * 3 * D + D + h * DH + d]); a = rb(rb(a) * 0.22360679774997896f); } else a = -1e9f;
    l[w] = a; mx = fmaxf(mx, a); }
  float sum = 0; for (int w = 0; w < 8; ++w) { l[w] = __expf(l[w] - mx); sum += l[w]; }
  float acc[DH] = {};
  for (int w = 0; w < 8; ++w) { int p = s - 2 * w; if (p < 0) p = 0; float a = rb(l[w] / sum);
    for (int d = 0; d < DH; ++d) acc[d] += a * bf2f(qkv[(size_t)p * 3 * D + 2 * D + h * DH + d]); }
  for (int d = 0; d < DH; ++d) o[(size_t)s * D + h * DH + d] = f2bf(acc[d]);
}

extern "C" void kernel_launch(void* const* d_in, const int* in_sizes, int n_in, void* d_out, int out_size, void* d_ws, size_t ws_size, hipStream_t stream) {
  const u16* x = (const u16*)d_in[0]; const u16* wqkv = (const u16*)d_in[1]; const u16* wo = (const u16*)d_in[2];
  const u16* w1 = (const u16*)d_in[4]; const u16* w2 = (const u16*)d_in[5]; const u16* w3 = (const u16*)d_in[6]; const u16* w4 = (const u16*)d_in[7];
  u16* ws = (u16*)d_ws; size_t SD = (size_t)S * D;
  u16 *xn = ws, *xnn = ws + SD, *qkv = ws + 2 * SD, *o = ws + 5 * SD, *s = ws + 6 * SD, *h = ws + 7 * SD, *x1 = ws + 8 * SD, *x1n = ws + 9 * SD, *h3 = ws + 10 * SD;
  ln_kernel<<<S / 4, 256, 0, stream>>>(x, xn, xnn);
  gemm_kernel<0><<<dim3(3 * D / 64, S / 64), 256, 0, stream>>>(xn, wqkv, qkv, 3 * D, D, nullptr, nullptr, nullptr);
  attn_kernel<<<S * H / 256, 256, 0, stream>>>(qkv, o);
  gemm_kernel<0><<<dim3(D / 64, S / 64), 256, 0, stream>>>(o, wo, s, D, D, nullptr, nullptr, nullptr);
  gemm_kernel<1><<<dim3(D / 64, S / 64), 256, 0, stream>>>(xnn, w1, h, D, D, nullptr, nullptr, nullptr);
  gemm_kernel<2><<<dim3(D / 64, S / 64), 256, 0, stream>>>(h, w2, x1, D, D, xn, nullptr, nullptr);
  ln_kernel<<<S / 4, 256, 0, stream>>>(x1, x1n, nullptr);
  gemm_kernel<1><<<dim3(F / 64, S / 64), 256, 0, stream>>>(x1n, w3, h3, F, D, nullptr, nullptr, nullptr);
  gemm_kernel<3><<<dim3(D / 64, S / 64), 256, 0, stream>>>(h3, w4, (u16*)d_out, D, F, x1, s, x);
}
```

```cpp
#include <hip/hip_runtime.h>
#include <stdint.h>

namespace {

typedef unsigned short u16;
typedef unsigned long long u64;
typedef unsigned v4u __attribute__((ext_vector_type(4)));
typedef unsigned v2u __attribute__((ext_vector_type(2)));
typedef float f4 __attribute__((ext_vector_type(4)));
typedef float f2 __attribute__((ext_vector_type(2)));
typedef __bf16 bf8 __attribute__((ext_vector_type(8)));
typedef __bf16 bf2 __attribute__((ext_vector_type(2)));
typedef short s4 __attribute__((ext_vector_type(4)));
typedef __amdgpu_buffer_rsrc_t rsrc_t;
#define GAS __attribute__((address_space(1)))
#define LAS __attribute__((address_space(3)))
#define DI __device__ __forceinline__
#define RLX_AGENT __ATOMIC_RELAXED, __HIP_MEMORY_SCOPE_AGENT

constexpr int S_ = 2048, D_ = 640, F_ = 1728;
constexpr float EPS = 1e-5f;
constexpr int SC1 = 16;
constexpr unsigned OOB = 0x7f000000u;

constexpr int W_EPOCH = 0, W_ARR = 1024;
constexpr int F_G1A = 4096, F_G1B = F_G1A + 16384, F_G2 = F_G1B + 16384, F_H = F_G2 + 16384, F_X1 = F_H + 8192, F_O = F_X1 + 8192,
              F_H3 = F_O + 8192;
constexpr int WS_S1 = 131072, S1_SL = 200 * 1024;
constexpr int WS_W3 = WS_S1 + 16 * S1_SL, W3_SL = 140 * 1024;
constexpr int WS_G2 = WS_W3 + 16 * W3_SL, G2_SL = 472 * 1024;
constexpr int WS_HS = WS_G2 + 8 * G2_SL, RT_D = 20 * 1024;
constexpr int WS_X1 = WS_HS + 128 * RT_D, WS_O = WS_X1 + 128 * RT_D, WS_H3 = WS_O + 128 * RT_D, RT_F = 54 * 1024;
constexpr int WS_ST = WS_H3 + 128 * RT_F, WS_END = WS_ST + 128 * 1024;

constexpr int L_PP2 = 6 * 4096, L_XST = 21 * 4096;
constexpr int L_HALO = 25 * 4096, L_KVC = L_HALO, L_QS = L_HALO + 20480, L_KVB = L_QS + 128 * 88, L_XS = L_KVB + 144 * 104;
constexpr int L_PP1 = L_QS;
constexpr int L_MISC = 39 * 4096, L_STAT = L_MISC, L_CS = L_STAT + 144 * 8, L_CNT = L_CS + 4 * 448, L_DUM = L_CNT + 64;
constexpr int LDS_BYTES = 160 * 1024;
static_assert(L_XS + 10240 <= L_MISC && L_DUM + 256 <= LDS_BYTES && L_PP1 + 16384 <= L_XS, "LDS map");

constexpr int ch_start(int n) {
  return n <= 5 ? 12 * n : n <= 10 ? 60 + 16 * (n - 5) : n <= 15 ? 140 + 12 * (n - 10) : n <= 20 ? 200 + 20 * (n - 15)
       : n <= 25 ? 300 + 28 * (n - 20) : n <= 30 ? 440 + 20 * (n - 25) : n < 44 ? 540 + 20 * (n - 30) : 810;
}
constexpr int ch_tiles(int n) { return n < 5 ? 3 : n < 10 ? 4 : n < 15 ? 3 : n < 20 ? 5 : n < 25 ? 7 : 5; }
constexpr bool ch_last(int n) { return n == 4 || n == 9 || n == 14 || n == 19 || n == 24 || n == 29 || n == 43; }
constexpr int upos(int u) { return u < 100 ? u % 25 : (u - 75) % 39; }
constexpr int fpos(int f) { return upos(f >> 2) * 4096 + (f & 3) * 1024; }
constexpr int cmin(int a, int b) { return a < b ? a : b; }
constexpr int lim_a(int n) { return cmin(100, ch_start(n) / 4 + 25); }
constexpr int lim_b(int n) { return cmin(203, ch_start(n) / 4 + 39); }
constexpr int ISS[45] = {14, 20, 23, 28, 28, 31, 38, 45, 50, 50, 50, 55, 60, 65, 70, 75, 80, 85, 90, 95,
                         114, 114, 118, 124, 130, 149, 154, 159, 164, 169, 174, 176, 179, 182, 186, 190, 195, 200, 203, 203, 203, 203, 203, 203, 8};
constexpr int need(int n) { return (ch_start(n + 1) + (ch_last(n) ? 0 : ch_tiles(n)) + 3) / 4; }

constexpr bool iss_ok() {
  for (int n = 0; n < 43; ++n)
    if (ISS[n] > (n < 20 ? lim_a(n) : lim_b(n)) || ISS[n] < need(n + 1) || (n && ISS[n] < ISS[n - 1])) return false;
  return ISS[44] >= need(0);
}
static_assert(iss_ok(), "ring schedule");

DI float bflo(unsigned u) { return __builtin_bit_cast(float, u << 16); }
DI float bfhi(unsigned u) { return __builtin_bit_cast(float, u & 0xffff0000u); }
DI unsigned pack(float a, float b) { bf2 r = {(__bf16)a, (__bf16)b}; return __builtin_bit_cast(unsigned, r); }
DI float dot2(unsigned a, unsigned b, float c) {
  return __builtin_amdgcn_fdot2_f32_bf16(__builtin_bit_cast(bf2, a), __builtin_bit_cast(bf2, b), c, false);
}
DI unsigned ones2() { unsigned o = 0x3f803f80u; asm volatile("" : "+v"(o)); return o; }
DI f4 mfma(v4u w, v4u x, f4 c) {
  return __builtin_amdgcn_mfma_f32_16x16x32_bf16(__builtin_bit_cast(bf8, w), __builtin_bit_cast(bf8, x), c, 0, 0, 0);
}
DI void mma(f4& c, v4u w, v4u x, bool first) {
  if (first) asm volatile("v_mfma_f32_16x16x32_bf16 %0, %1, %2, 0" : "=&v"(c) : "v"(w), "v"(x));
  else asm volatile("v_mfma_f32_16x16x32_bf16 %0, %1, %2, %0" : "+v"(c) : "v"(w), "v"(x));
}
#define ACC_DONE() asm volatile("s_nop 7\n\ts_nop 1" ::: "memory")
DI float asf(unsigned u) { return __builtin_bit_cast(float, u); }
DI unsigned asu(float f) { return __builtin_bit_cast(unsigned, f); }
DI float sum_g(float v) {
  const auto a = __builtin_amdgcn_permlane16_swap(asu(v), asu(v), false, false);
  const float s = asf(a[0]) + asf(a[1]);
  const auto b = __builtin_amdgcn_permlane32_swap(asu(s), asu(s), false, false);
  return asf(b[0]) + asf(b[1]);
}
template <int CTRL> DI unsigned dpp(unsigned v) { return __builtin_amdgcn_update_dpp(0u, v, CTRL, 0xf, 0xf, true); }
template <int CTRL> DI float dppf(float v) { return __builtin_bit_cast(float, dpp<CTRL>(__builtin_bit_cast(unsigned, v))); }
struct Gelu {
  float c0, c1;
  DI Gelu() : c0(-2.302208198f), c1(-0.1029432397f) { asm volatile("" : "+v"(c0), "+v"(c1)); }
  DI f4 operator()(f4 v) const {
    const f4 t = v * (v * v * c1 + c0);
    const f4 d = f4{__builtin_amdgcn_exp2f(t[0]), __builtin_amdgcn_exp2f(t[1]), __builtin_amdgcn_exp2f(t[2]), __builtin_amdgcn_exp2f(t[3])} + 1.0f;
    return v * f4{__builtin_amdgcn_rcpf(d[0]), __builtin_amdgcn_rcpf(d[1]), __builtin_amdgcn_rcpf(d[2]), __builtin_amdgcn_rcpf(d[3])};
  }
};
DI v2u pack4(f4 v) { return v2u{pack(v[0], v[1]), pack(v[2], v[3])}; }
DI v4u widen(v2u v) {
  const auto sx = __builtin_amdgcn_permlane16_swap(v.x, v.x, false, false), sy = __builtin_amdgcn_permlane16_swap(v.y, v.y, false, false);
  return v4u{sx[0], sy[0], sx[1], sy[1]};
}
DI f4 unpack4(v2u u) { return f4{bflo(u.x), bfhi(u.x), bflo(u.y), bfhi(u.y)}; }

DI int lane_id() {
  int l;
  asm volatile("v_mbcnt_lo_u32_b32 %0, -1, 0\n\tv_mbcnt_hi_u32_b32 %0, -1, %0" : "=v"(l));
  return l;
}
#define LANES() const int lane = lane_id(), i16 = lane & 15, g = lane >> 4, lane16 = lane * 16; (void)i16; (void)g; (void)lane16
#define SB0() __builtin_amdgcn_sched_barrier(0)
#define BAR() do { SB0(); asm volatile("s_barrier" ::: "memory"); SB0(); } while (0)
#define WAIT_VM(n) asm volatile("s_waitcnt vmcnt(%0)" ::"n"(n) : "memory")
#define WAIT_LGKM0() asm volatile("s_waitcnt lgkmcnt(0)" ::: "memory")

typedef LAS char* lptr;
DI lptr lbase(int off) { asm volatile("" : "+v"(off)); return (lptr)(size_t)(unsigned)off; }
DI v4u lds128(lptr a, int o = 0) { return *(const LAS v4u*)(a + o); }
DI v2u lds64(lptr a, int o = 0) { return *(const LAS v2u*)(a + o); }
DI unsigned lds32(lptr a, int o = 0) { return *(const LAS unsigned*)(a + o); }
DI f4 ldsf4(lptr a, int o = 0) { return *(const LAS f4*)(a + o); }
DI f2 ldsf2(lptr a, int o = 0) { return *(const LAS f2*)(a + o); }
DI void sts128(lptr a, int o, v4u v) { *(LAS v4u*)(a + o) = v; }
DI void sts64(lptr a, int o, v2u v) { *(LAS v2u*)(a + o) = v; }
DI void stsf2(lptr a, int o, f2 v) { *(LAS f2*)(a + o) = v; }
DI void stsf(lptr a, int o, float v) { *(LAS float*)(a + o) = v; }
DI v2u lds_tr(lptr a, int o = 0) { return __builtin_bit_cast(v2u, __builtin_amdgcn_ds_read_tr16_b64_v4i16((LAS s4*)(a + o))); }
struct Ring {
  lptr b0, b1, b2;
  DI Ring(int lane16) : b0(lbase(lane16)), b1(lbase(lane16 + 65536)), b2(lbase(lane16 + 131072)) {}
  DI v4u operator()(int f) const { const int a = fpos(f); return lds128(a < 65536 ? b0 : a < 131072 ? b1 : b2, a & 65535); }
};

DI void dma(rsrc_t r, unsigned voff, unsigned soff, unsigned lds) {
  asm volatile("s_nop 4\n\ts_mov_b32 m0, %0\n\ts_nop 0\n\tbuffer_load_dwordx4 %1, %2, %3 offen lds" ::"s"(lds), "v"(voff), "s"(r), "s"(soff) : "memory");
}
DI void dma_sc1(rsrc_t r, unsigned voff, unsigned soff, unsigned lds) {
  asm volatile("s_mov_b32 m0, %0\n\ts_nop 0\n\tbuffer_load_dwordx4 %1, %2, %3 offen sc1 lds" ::"s"(lds), "v"(voff), "s"(r), "s"(soff) : "memory");
}
DI v4u ld128(rsrc_t r, unsigned voff, unsigned soff) { return __builtin_amdgcn_raw_buffer_load_b128(r, voff, soff, SC1); }
DI void st128(v4u v, rsrc_t r, unsigned voff, unsigned soff) { __builtin_amdgcn_raw_buffer_store_b128(v, r, voff + soff, 0, SC1); }
DI void st64(v2u v, rsrc_t r, unsigned voff, unsigned soff) { __builtin_amdgcn_raw_buffer_store_b64(v, r, voff, soff, SC1); }

DI void flag_set(char* ws, int off, u64 tag) { (void)__hip_atomic_exchange((GAS u64*)(ws + off), tag, RLX_AGENT); }
DI void flag_wait2(char* ws, int off, u64 tag) {
  const GAS u64* p = (const GAS u64*)(ws + off);
  for (int spin = 0; spin < (1 << 18); ++spin) {
    const u64 v = __hip_atomic_load(p, RLX_AGENT);
    if (__all(v == tag)) break;
  }
  __builtin_amdgcn_fence(__ATOMIC_ACQUIRE, "wavefront");
  asm volatile("" ::: "memory");
}
DI void flag_wait(char* ws, int off, int n, u64 tag, int lane) { flag_wait2(ws, off + (lane < n ? lane : 0) * 8, tag); }

struct Params {
  const u16 *x, *wqkv, *wo, *w1, *w2, *w3, *w4;
  u16* out;
  char* ws;
};

#define BSTAMP(i)
#define STAMP(i)
#define CSTAMP(i)
#define STAMP_OUT()

template <int NM, int ND> DI void interleave() {
#pragma unroll
  for (int i = 0; i < NM; ++i) {
    __builtin_amdgcn_sched_group_barrier(0x8, 1, 0);
    if (i < ND) __builtin_amdgcn_sched_group_barrier(0x100, (ND + NM - 1) / NM, 0);
  }
}

template <int U0, int U1> DI void ring_issue(rsrc_t rw, unsigned lane16, unsigned sS1, unsigned sG2, unsigned sW3, unsigned lw1k) {
#pragma unroll
  for (int u = U0; u < U1; ++u) {
    const int f = 4 * u;
    const unsigned so = f < 200 ? sS1 + f * 1024 : f < 300 ? sG2 + (f - 200) * 1024 : f < 440 ? sW3 + (f - 300) * 1024 : sG2 + (f - 340) * 1024;
    dma_sc1(rw, lane16, so, lw1k + upos(u) * 4096);
  }
}
template <int N, int NT> DI void colsum_step(f4 (&cs)[2], v4u one, int lane16, int lw) {
  constexpr int F0 = ch_start(N);
  static_assert(fpos(F0 + 2 * NT - 1) == fpos(F0) + (2 * NT - 1) * 1024 && fpos(F0 + 4 * NT - 1) == fpos(F0 + 2 * NT) + (2 * NT - 1) * 1024, "wrap");
  const int t0 = (lw < NT ? lw : NT - 1) * 1024, t1 = (lw + 4 < NT ? lw + 4 : NT - 1) * 1024;
  const lptr a = lbase(lane16 + fpos(F0) + t0), b = lbase(lane16 + fpos(F0 + 2 * NT) + t0);
#pragma unroll
  for (int k = 0; k < 4; ++k) {
    cs[0] = mfma(lds128(k < 2 ? a : b, (k & 1) * NT * 1024), one, cs[0]);
    if (NT > 4) cs[1] = mfma(lds128((k < 2 ? a : b) + (t1 - t0), (k & 1) * NT * 1024), one, cs[1]);
  }
}
template <int NT> DI void colsum_flush(f4 (&cs)[2], int lane, int lw) {
  if ((lane & 15) == 0) {
    if (lw < NT) *(LAS f4*)(size_t)(L_CS + lw * 64 + lane) = cs[0];
    if (lw + 4 < NT) *(LAS f4*)(size_t)(L_CS + (lw + 4) * 64 + lane) = cs[1];
  }
  cs[0] = cs[1] = f4{0.f, 0.f, 0.f, 0.f};
  WAIT_LGKM0();
}
DI void colsum_put(f4 v, int t) {
  const int lane = lane_id();
  *(LAS f4*)lbase((lane & 15) == 0 ? L_CS + t * 64 + lane : L_DUM) = v;
  WAIT_LGKM0();
}
DI f4 colsum_get(lptr a, int t) { return ldsf4(a, t * 64); }
DI int sel(bool c, int a, int b) { asm volatile("" : "+v"(a)); return c ? a : b; }

__global__ __launch_bounds__(512, 2) void mk_kernel(Params p) {
  extern __shared__ __attribute__((aligned(16))) char smem_[];
  u64 pc; unsigned len;
  asm volatile("s_getpc_b64 %0\nmk_code_from:\n\ts_mov_b32 %1, mk_code_to-mk_code_from" : "=s"(pc), "=s"(len));
  const int tid = threadIdx.x, lane = tid & 63, wv = __builtin_amdgcn_readfirstlane(tid >> 6);
  const int b = blockIdx.x, P = ((b & 7) >> 1) * 4 + (b >> 6), j = (b & 1) * 8 + ((b >> 3) & 7);
  const int half = j >> 3, c = j & 7;
  const int i16 = lane & 15, g = lane >> 4, lane16 = lane * 16;
  const rsrc_t rw = __builtin_amdgcn_make_buffer_rsrc(p.ws, 0, WS_END, 0x00020000);
  const rsrc_t rx = __builtin_amdgcn_make_buffer_rsrc((void*)p.x, 0, S_ * D_ * 2, 0x00020000);
  const int s3 = (27 * j) >> 2;
  asm volatile("" ::"v"(smem_));

  v2u xs[5], x1p[5];
  u64 tag = 0;
  STAMP(0); CSTAMP(30);

  if (wv < 4) {
    const int w = wv;
    __builtin_amdgcn_s_setprio(1);
    v4u xf[2][20];
    float m1[2], r1[2], r12[2];
    {
      v4u xr[10][4];
      const unsigned vo = (lane >> 3) * 1280 + (lane & 7) * 16;
      if (w == 0 && lane < 16) {
        const rsrc_t rc = __builtin_amdgcn_make_buffer_rsrc((void*)pc, 0, len, 0x00020000);
        asm volatile("s_nop 4\n\ts_mov_b32 m0, %0\n\ts_nop 0\n\tbuffer_load_dword %1, %2, 0 offen lds" ::"s"(L_DUM), "v"((b >> 3) * 2048 + lane * 128), "s"(rc) : "memory");
      }
      BAR();
#pragma unroll
      for (int ch = 0; ch < 10; ++ch) {
        if (ch == 4) BAR();
#pragma unroll
        for (int q = 0; q < 4; ++q)
          xr[ch][q] = __builtin_amdgcn_raw_buffer_load_b128(rx, vo + ch * 128, (128 * P + 32 * w + 8 * q) * 1280, 0);
      }
      SB0();
      const int wa = L_XST + w * 4096 + (lane >> 3) * 128 + (((lane & 7) ^ (lane >> 4)) * 16);
      const int ra = L_XST + w * 4096 + i16 * 128 + ((g ^ (i16 >> 1)) * 16);
      const lptr wa0 = lbase(wa), wa1 = lbase(wa ^ 64), ra0 = lbase(ra), ra1 = lbase(ra ^ 64);
      f4 sm[2] = {}, sq[2] = {};
      const unsigned o1 = ones2();
      const v4u one = {o1, o1, o1, o1};
#pragma unroll
      for (int ch = 0; ch <= 10; ++ch) {
        if (ch < 10) {
#pragma unroll
          for (int q = 0; q < 4; ++q) sts128(q & 1 ? wa1 : wa0, q * 1024, xr[ch][q]);
#pragma unroll
          for (int rt = 0; rt < 2; ++rt)
#pragma unroll
            for (int ks = 0; ks < 2; ++ks) xf[rt][2 * ch + ks] = lds128(ks ? ra1 : ra0, rt * 2048);
        }
        SB0();
        if (ch > 0)
#pragma unroll
          for (int ks = 0; ks < 2; ++ks)
#pragma unroll
            for (int rt = 0; rt < 2; ++rt) {
              sm[rt] = mfma(one, xf[rt][2 * ch - 2 + ks], sm[rt]);
              sq[rt] = mfma(xf[rt][2 * ch - 2 + ks], xf[rt][2 * ch - 2 + ks], sq[rt]);
            }
        SB0();
      }
      const lptr sa = lbase(L_STAT + (16 + 32 * w + i16) * 8);
#pragma unroll
      for (int rt = 0; rt < 2; ++rt) {
        const float m = sm[rt][0] * (1.0f / D_);
        const float d01 = (lane & 1) ? sq[rt][1] : sq[rt][0], d23 = (lane & 1) ? sq[rt][3] : sq[rt][2], dg = (lane & 2) ? d23 : d01;
        const float var = fmaxf(sum_g(g == (i16 >> 2) ? dg : 0.f) * (1.0f / D_) - m * m, 0.f);
        const float r = __builtin_amdgcn_rsqf(var + EPS);
        m1[rt] = m; r1[rt] = r; r12[rt] = r * __builtin_amdgcn_rsqf(var * r * r + EPS);
        stsf2(sa, rt * 128, f2{m, r});
      }
      WAIT_LGKM0();
    }
    STAMP(1);
    BAR();
    STAMP(2);

    const Ring ring(lane16);
    v4u wb[4], hw, hx;
    {
      f4 acc[2][3], hacc;
      v4u wf[2][3];
      const int tsel = w < 2 ? w : 2, hxa = L_HALO + (i16 >> 2) * 5120 + (i16 & 3) * 128 + ((g ^ (i16 >> 1)) * 16);
      static_assert(fpos(59) == 59 * 1024, "phase a is contiguous in the ring");
      const lptr hwa = lbase(lane16 + tsel * 1024), hx0 = lbase(hxa), hx1 = lbase(hxa ^ 64);
      hw = lds128(hwa); hx = lds128(hx0);
#pragma unroll
      for (int t = 0; t < 3; ++t) wf[0][t] = ring(t);
      SB0();
#pragma unroll
      for (int kb = 0; kb < 20; ++kb) {
        if (kb && (kb & 3) == 0) BAR();
        mma(hacc, hw, hx, kb == 0);
        SB0();
        if (kb + 1 < 20) hw = lds128(hwa, 3 * (kb + 1) * 1024);
        SB0();
#pragma unroll
        for (int t = 0; t < 3; ++t) {
          mma(acc[0][t], wf[kb & 1][t], xf[0][kb], kb == 0);
          SB0();
          if (kb + 1 < 20) wf[(kb + 1) & 1][t] = ring(3 * (kb + 1) + t);
          SB0();
          mma(acc[1][t], wf[kb & 1][t], xf[1][kb], kb == 0);
          SB0();
          if (t == 0 && kb + 1 < 20) hx = lds128((kb + 1) & 1 ? hx1 : hx0, ((kb + 1) >> 1) * 512);
          SB0();
        }
      }
      ACC_DONE();
      colsum_put(hacc, tsel);
      BAR();
      STAMP(3);
      {
      LANES();
      const lptr ca = lbase(L_CS + 16 * g);
      const Gelu gelu;
      f4 cs[3];
#pragma unroll
      for (int t = 0; t < 3; ++t) cs[t] = colsum_get(ca, t);
#pragma unroll
      for (int t = 0; t < 4; ++t) wb[t] = ring(60 + t);
      const lptr qa = lbase(sel(g >= 2, L_QS + (32 * w + i16) * 88 + (g - 2) * 8, L_DUM)), qb = lbase(sel(g >= 2, L_QS + (32 * w + 16 + i16) * 88 + (g - 2) * 8, L_DUM));
#pragma unroll
      for (int rt = 0; rt < 2; ++rt) {
        const unsigned base = WS_HS + (8 * P + 2 * w + rt) * RT_D;
        const f4 c2 = acc[rt][2] - m1[rt] * cs[2];
        const v2u h0 = pack4(gelu((acc[rt][0] - m1[rt] * cs[0]) * r12[rt])), h1 = pack4(gelu((acc[rt][1] - m1[rt] * cs[1]) * r12[rt]));
        st128(v4u{h0.x, h0.y, h1.x, h1.y}, rw, lane16, base + j * 1024);
        st64(pack4(gelu(c2 * r12[rt])), rw, g < 2 ? (unsigned)(i16 * 16 + g * 8) : OOB, base + (16 + (j >> 2)) * 1024 + (j & 3) * 256);
        sts64(rt ? qb : qa, 0, pack4(c2 * r1[rt]));
      }
      }
      SB0();
    }
    STAMP(4);
    {
      f4 acc[2][4], hacc;
      LANES();
      const int tsel = w, hxa = L_HALO + (i16 >> 2) * 5120 + (i16 & 3) * 128 + ((g ^ (i16 >> 1)) * 16);
      const lptr hwa0 = lbase(lane16 + tsel * 1024), hwa1 = lbase(lane16 + tsel * 1024 + 65536), hx0 = lbase(hxa), hx1 = lbase(hxa ^ 64);
      hw = lds128(hwa0, fpos(60)); hx = lds128(hx0);
      SB0();
#pragma unroll
      for (int kb = 0; kb < 20; ++kb) {
        if (kb == 8) WAIT_VM(0);
        if (kb && (kb & 3) == 0) BAR();
#pragma unroll
        for (int t = 0; t < 4; ++t) {
#pragma unroll
          for (int rt = 0; rt < 2; ++rt) mma(acc[rt][t], wb[t], xf[rt][kb], kb == 0);
          SB0();
          if (kb + 1 < 20) wb[t] = ring(60 + 4 * (kb + 1) + t);
          SB0();
        }
        mma(hacc, hw, hx, kb == 0);
        SB0();
        if (kb + 1 < 20) {
          const int a = fpos(60 + 4 * (kb + 1));
          hw = lds128(a < 63488 ? hwa0 : hwa1, a < 63488 ? a : a - 65536);
          hx = lds128((kb + 1) & 1 ? hx1 : hx0, ((kb + 1) >> 1) * 512);
        }
        SB0();
      }
      ACC_DONE();
      colsum_put(hacc, tsel);
      BAR();
      STAMP(5);
      {
      LANES();
      const lptr ca = lbase(L_CS + 16 * g);
      const lptr qa = lbase(L_QS + (32 * w + i16) * 88 + (2 + g) * 8), ka = lbase(L_KVB + (16 + 32 * w + i16) * 104 + g * 8);
      f4 cs[4];
#pragma unroll
      for (int t = 0; t < 4; ++t) cs[t] = colsum_get(ca, t);
      const f4 hcs = colsum_get(ca + tsel * 64, 0);
      const f2 st = ldsf2(lbase(L_STAT + i16 * 8));
#pragma unroll
      for (int t = 0; t < 3; ++t) wb[t] = ring(140 + t);
      SB0();
#pragma unroll
      for (int t = 0; t < 4; ++t) {
#pragma unroll
        for (int rt = 0; rt < 2; ++rt) {
          const v2u v = pack4((acc[rt][t] - m1[rt] * cs[t]) * r1[rt]);
          if (t < 2) sts64(qa, rt * 16 * 88 + t * 32, v);
          else sts64(ka, rt * 16 * 104 + (t - 2) * 32, v);
        }
      }
      sts64(lbase(sel(w >= 2, L_KVB + i16 * 104 + (4 * (w - 2) + g) * 8, L_DUM)), 0, pack4((hacc - st.x * hcs) * st.y));
      }
      SB0();
    }
    STAMP(6);
    v4u hf[20], w5[2][5];
    {
      f4 acc[2][3], hacc;
      v4u wf[2][3];
      LANES();
      const int tsel = w < 2 ? w : 2, hxa = L_HALO + (i16 >> 2) * 5120 + (i16 & 3) * 128 + ((g ^ (i16 >> 1)) * 16);
      static_assert(fpos(140) == 40960 && fpos(199) == fpos(140) + 59 * 1024, "phase c is contiguous in the ring");
      const lptr hwa = lbase(lane16 + tsel * 1024 + fpos(140)), hx0 = lbase(hxa), hx1 = lbase(hxa ^ 64);
      unsigned hv[4];
#pragma unroll
      for (int q = 0; q < 4; ++q) {
        const int r = 128 * P + 64 * half + 16 * w + i16 + (q & 1 ? 1 : -1) * (1 + (q >> 1));
        hv[q] = sel((unsigned)r < (unsigned)S_, (r >> 4) * RT_D + g * 256 + (r & 15) * 16, OOB);
      }
#pragma unroll
      for (int t = 0; t < 3; ++t) wf[0][t] = wb[t];
      hw = lds128(hwa); hx = lds128(hx0);
      SB0();
#pragma unroll
      for (int kb = 0; kb < 20; ++kb) {
        if (kb && (kb & 3) == 0) BAR();
        mma(hacc, hw, hx, kb == 0);
        SB0();
        if (kb + 1 < 20) hw = lds128(hwa, 3 * (kb + 1) * 1024);
        SB0();
#pragma unroll
        for (int t = 0; t < 3; ++t) {
          mma(acc[0][t], wf[kb & 1][t], xf[0][kb], kb == 0);
          SB0();
          if (kb + 1 < 20) wf[(kb + 1) & 1][t] = ring(140 + 3 * (kb + 1) + t);
          SB0();
          mma(acc[1][t], wf[kb & 1][t], xf[1][kb], kb == 0);
          SB0();
          if (t == 0 && kb + 1 < 20) hx = lds128((kb + 1) & 1 ? hx1 : hx0, ((kb + 1) >> 1) * 512);
          SB0();
        }
        if (kb >= 4 && kb < 14) {
#pragma unroll
          for (int k2 = 2 * (kb - 4); k2 < 2 * (kb - 4) + 2; ++k2)
            hf[k2] = ld128(rw, hv[k2 < 16 ? k2 >> 2 : k2 - 16] + (k2 & 3) * 1024, WS_HS + (k2 & ~3) * 1024);
          SB0();
        }
      }
      ACC_DONE();
      colsum_put(hacc, tsel);
      BAR();
      STAMP(7);
      {
      LANES();
      const lptr ca = lbase(L_CS + 16 * g), ka = lbase(L_KVC + (16 + 32 * w + i16) * 104 + g * 8);
      f4 cs[3];
#pragma unroll
      for (int t = 0; t < 3; ++t) cs[t] = colsum_get(ca, t);
      const f4 hcs = colsum_get(ca + tsel * 64, 0);
      const f2 st = ldsf2(lbase(L_STAT + i16 * 8));
#pragma unroll
      for (int t = 0; t < 5; ++t) w5[0][t] = ring(200 + t);
      SB0();
#pragma unroll
      for (int t = 0; t < 3; ++t)
#pragma unroll
        for (int rt = 0; rt < 2; ++rt) sts64(ka, rt * 16 * 104 + t * 32, pack4((acc[rt][t] - m1[rt] * cs[t]) * r1[rt]));
      sts64(lbase(sel(w < 3, L_KVC + i16 * 104 + (4 * w + g) * 8, L_DUM)), 0, pack4((hacc - st.x * hcs) * st.y));
      }
      SB0();
    }
    STAMP(8);
    {
      f4 acc[5];
#pragma unroll
      for (int kb = 0; kb < 20; ++kb) {
        if (kb && (kb & 3) == 0) BAR();
#pragma unroll
        for (int t = 0; t < 5; ++t) {
          mma(acc[t], w5[kb & 1][t], hf[kb], kb == 0);
          SB0();
          if (kb + 1 < 20) w5[(kb + 1) & 1][t] = ring(200 + 5 * (kb + 1) + t);
          SB0();
        }
      }
      ACC_DONE();
      BAR();
      STAMP(9);
      LANES();
      const f2 st = ldsf2(lbase(L_STAT + (16 + 64 * half + 16 * w + i16) * 8));
      const lptr xa = lbase(L_XS + (16 * w + i16) * 160 + g * 8 + (c & 1) * 32), xl = lbase(L_XS + (16 * w + i16) * 160 + g * 8 + ((c & 1) ? 0 : 128));
      f4 v[5];
      float sm = 0.f;
#pragma unroll
      for (int t = 0; t < 5; ++t) {
        xs[t] = t < 4 ? lds64(xa, 32 * t) : lds64(xl);
        x1p[t] = pack4((unpack4(xs[t]) - st.x) * st.y + acc[t]);
        v[t] = unpack4(x1p[t]);
        sm += (v[t][0] + v[t][1]) + (v[t][2] + v[t][3]);
      }
      const unsigned rb = (8 * P + 4 * half + w), kb0 = (5 * c + 1) >> 1, kbl = (c & 1) ? (5 * c) >> 1 : (5 * c + 4) >> 1;
      st128(v4u{x1p[0].x, x1p[0].y, x1p[1].x, x1p[1].y}, rw, lane16, WS_X1 + rb * RT_D + kb0 * 1024);
      st128(v4u{x1p[2].x, x1p[2].y, x1p[3].x, x1p[3].y}, rw, lane16, WS_X1 + rb * RT_D + kb0 * 1024 + 1024);
      st128(widen(x1p[4]), rw, (g & 1) ? OOB : (unsigned)((g >> 1) * 256 + i16 * 16), WS_X1 + rb * RT_D + kbl * 1024 + (c & 1) * 512);
      const float mp = sum_g(sm) * (1.0f / 80);
      float m2 = 0.f;
#pragma unroll
      for (int t = 0; t < 5; ++t) { const f4 d = v[t] - mp; m2 += (d[0] * d[0] + d[1] * d[1]) + (d[2] * d[2] + d[3] * d[3]); }
      m2 = sum_g(m2);
      st64(v2u{__builtin_bit_cast(unsigned, mp), __builtin_bit_cast(unsigned, m2)}, rw, g == 0 ? (unsigned)(lane * 8) : OOB, WS_ST + rb * 1024 + c * 128);
    }
    STAMP(10);
  } else {
    const int lw = wv - 4;
    __builtin_amdgcn_s_setprio(3);
    u64 ep;
    asm volatile("global_load_dwordx2 %0, %1, off sc1" : "=v"(ep) : "v"(p.ws + W_EPOCH) : "memory");
    {
      const rsrc_t r = lw == 0 ? __builtin_amdgcn_make_buffer_rsrc((void*)p.w1, 0, D_ * D_ * 2, 0x00020000)
                               : __builtin_amdgcn_make_buffer_rsrc((void*)p.wqkv, 0, D_ * 3 * D_ * 2, 0x00020000);
      const int stride = lw == 0 ? 1280 : 3840, co = (lw == 0 ? 0 : (lw - 1) * 1280) + 80 * j;
#pragma unroll
      for (int it = 0; it < 4; ++it) {
        const int idx = it * 64 + lane, row = idx / 5, pc = idx - row * 5;
        dma(r, idx < 200 ? (unsigned)(row * stride + pc * 16) : OOB, 40 * P * stride + co, L_PP1 + lw * 4096 + it * 1024);
      }
    }
    if (tid == 256) *(LAS unsigned*)(size_t)L_CNT = 0u;
    WAIT_LGKM0();
    BAR();
    {
      lptr ta[4];
      unsigned to[4], hv;
#pragma unroll
      for (int sl = 0; sl < 4; ++sl) {
        const int un = sl * 4 + g;
        int ps = un * 4 + (lane & 3); ps = ps < 50 ? ps : 49;
        const int cs_ = ps / 10, gs = ps - 10 * cs_;
        ta[sl] = lbase(L_PP1 + lw * 4096 + (8 * cs_ + ((lane >> 2) & 3)) * 80 + gs * 8);
        const int pr = un * 4 + ((lane >> 2) & 3), cr = pr / 10, G = 10 * lw + pr - 10 * cr, T = G >> 2, cc = 5 * P + cr;
        const int f = T < 3 ? 3 * (cc >> 2) + T : T < 7 ? 60 + 4 * (cc >> 2) + T - 3 : 140 + 3 * (cc >> 2) + T - 7;
        to[sl] = pr < 50 ? (unsigned)(WS_S1 + j * S1_SL + f * 1024 + ((cc & 3) * 16 + (G & 3) * 4 + (lane & 3)) * 16) : OOB;
        asm volatile("" : "+v"(to[sl]));
      }
      {
        const int row = 4 * lw + ((lane >> 3) & 3);
        hv = ((128 * P - 16 + row) & (S_ - 1)) * 1280 + (lane >> 5) * 128 + (((lane & 7) ^ (row >> 1)) * 16);
        asm volatile("" : "+v"(hv));
      }
      asm volatile("s_waitcnt vmcnt(0)" : "+v"(ep)::"memory");
      STAMP(1);
      v2u lo[4], hi[4];
#pragma unroll
      for (int sl = 0; sl < 4; ++sl) { lo[sl] = lds_tr(ta[sl]); hi[sl] = lds_tr(ta[sl], 320); }
#pragma unroll
      for (int sl = 0; sl < 4; ++sl) st128(v4u{lo[sl].x, lo[sl].y, hi[sl].x, hi[sl].y}, rw, to[sl], 0);
      BAR();
#pragma unroll
      for (int it = 0; it < 5; ++it) dma(rx, hv, it * 256, L_HALO + lw * 5120 + it * 1024);
      const unsigned elo = __builtin_amdgcn_readfirstlane((unsigned)ep), ehi = __builtin_amdgcn_readfirstlane((unsigned)(ep >> 32));
      tag = (((u64)ehi << 32) | elo) + 1;
      STAMP(8);
      WAIT_VM(5);
      STAMP(9);
      unsigned old = 0;
      if (lane == 0) old = __hip_atomic_fetch_add((LAS unsigned*)(size_t)L_CNT, 1u, __ATOMIC_RELAXED, __HIP_MEMORY_SCOPE_WORKGROUP);
      if (__builtin_amdgcn_readfirstlane(old) == 3 && lane < 5)
        flag_set(p.ws, lane < 4 ? F_G1A + (j * 4 + lane) * 256 + P * 8 : W_ARR + b * 8, tag);
    }
    STAMP(2);
    const int kj = 3 * (2 * P + half) + lw;
    const bool j3 = lw == 3;
    unsigned sv[9], dv[9], xv[3];
    lptr tv[9];
    const rsrc_t r2 = j3      ? __builtin_amdgcn_make_buffer_rsrc((void*)p.w3, 0, D_ * F_ * 2, 0x00020000)
                    : kj < 20 ? __builtin_amdgcn_make_buffer_rsrc((void*)p.w2, 0, D_ * D_ * 2, 0x00020000)
                    : kj < 40 ? __builtin_amdgcn_make_buffer_rsrc((void*)p.wo, 0, D_ * D_ * 2, 0x00020000)
                              : __builtin_amdgcn_make_buffer_rsrc((void*)p.w4, 0, F_ * D_ * 2, 0x00020000);
    const int pp2w = L_PP2 + lw * 5120, prow = j3 ? 224 : 160;
    {
      const int e = lane & 7, ck = lane >> 3, cc = (j3 ? 5 * P : 4 * (kj - 20)) + ck, gg = cc & 3;
      int base, jump;
      if (j3 || kj >= 40) {
        const int kb = j3 ? cc >> 2 : kj - 40, km = j3 ? kb % 5 : kb % 27;
        const bool plain = j3 ? km == 2 : (km == 6 || km == 13 || km == 23);
        base = 32 * kb + (plain ? 8 : 4) * gg; jump = plain ? 0 : 12;
      } else if (kj < 16) { base = 40 * kj + 4 * gg; jump = 12; }
      else if (kj < 20) { base = 160 * (kj - 16) + 40 * gg + 32; jump = 0; }
      else {
        const int jj = cc / 5, pi = cc - 5 * jj;
        base = 40 * jj + (pi == 4 ? 8 : 20 * (pi >> 1) + 12 * (pi & 1)); jump = pi == 4 ? 16 : 0;
      }
      const int rowoff = (base + e + (e >= 4 ? jump : 0)) * (j3 ? 3456 : 1280) + (j3 ? 32 * s3 : 160 * c);
      const int W = j3 ? 14 : 10, M = j3 ? 4682 : 6554, NT = j3 ? 7 : 5, MT = j3 ? 9363 : 13108, nun = j3 ? 35 : kj < 94 ? 20 : 0;
      const int odd = j3 ? s3 & 1 : c & 1, c0 = j3 ? 5 * P : 0;
      const unsigned dbase = (j3 ? WS_W3 + j * W3_SL : WS_G2 + c * G2_SL + kj * 5120) + i16 * 16;
#pragma unroll
      for (int it = 0; it < 9; ++it) {
        const int idx = it * 64 + lane, r_ = (idx * M) >> 16;
        sv[it] = sel(idx < 560, __shfl(rowoff, r_) + (idx - r_ * W) * 16, OOB);
        const int un0 = it * 4 + g, un = un0 < 35 ? un0 : 34, cl = (un * MT) >> 16, t = un - NT * cl, c5 = c0 + cl;
        const int co = ((t + odd) == NT ? 0 : t + odd) * 32;
        tv[it] = lbase(pp2w + (8 * cl + ((lane >> 2) & 3)) * prow + co + (lane & 3) * 8);
        dv[it] = sel(un0 < nun, dbase + ((c5 >> 2) * NT + t) * 1024 + (c5 & 3) * 256, OOB);
        asm volatile("" : "+v"(sv[it]), "+v"(dv[it]));
      }
    }
#pragma unroll
    for (int it = 0; it < 3; ++it) {
      const int idx = it * 64 + lane, row = idx / 10;
      xv[it] = row * 1280 + (idx - row * 10) * 16;
      asm volatile("" : "+v"(xv[it]));
    }
    STAMP(3);
    const unsigned lw1k = lw * 1024, sS1 = WS_S1 + j * S1_SL + lw1k, sG2 = WS_G2 + c * G2_SL + lw1k, sW3 = WS_W3 + j * W3_SL + lw1k;
    flag_wait(p.ws, F_G1A + (j * 4 + (P >> 2)) * 256, 16, tag, lane);
    STAMP(4);
    ring_issue<0, ISS[44]>(rw, lane16, sS1, sG2, sW3, lw1k);
    if (lw == 0) {
      const unsigned o2 = ones2();
      sts128(lbase(L_HALO + (lane >> 3) * 512 + (lane & 7) * 16), 0, v4u{o2, o2, o2, o2});
      if (lane < 16) sts128(lbase(L_HALO + (8 + (lane >> 3)) * 512 + (lane & 7) * 16), 0, v4u{o2, o2, o2, o2});
    }
    WAIT_VM(ISS[44] - need(0));
    WAIT_LGKM0();
    STAMP(5); BSTAMP(32);
    BAR();
    BSTAMP(33);
    __builtin_amdgcn_s_setprio(0);
#define TICK(N) ring_issue<ISS[N ? N - 1 : 44], ISS[N]>(rw, lane16, sS1, sG2, sW3, lw1k)
#define LBAR(N) do { BSTAMP(32 + 2 * (N)); BAR(); BSTAMP(33 + 2 * (N)); } while (0)
#define TOCKX(N, X) do { WAIT_VM(ISS[N] - need(N + 1) + (X)); LBAR(N + 1); } while (0)
#define TOCK(N) TOCKX(N, 0)
    TICK(0); TOCK(0);
    TICK(1); TOCK(1);
#pragma unroll
    for (int it = 0; it < 3; ++it)
      if (it < 2 || lane < 32) dma(rx, xv[it], (128 * P + 64 * half + 16 * lw) * 1280 + 160 * c, L_XS + lw * 2560 + it * 1024);
    TICK(2); TOCKX(2, 3);
    TICK(3); TOCKX(3, 3);
    static_assert(ISS[5] <= 31 && ch_start(4) / 4 >= 12, "job 2 is staged in the ring slots of units 31..36");
#pragma unroll
    for (int it = 0; it < 5; ++it) dma(r2, sv[it], 0, pp2w + it * 1024);
    if (j3) {
#pragma unroll
      for (int it = 5; it < 9; ++it) dma(r2, sv[it], 0, pp2w + it * 1024);
      WAIT_VM(ISS[4] - need(5) + 3 + 9);
    } else WAIT_VM(ISS[4] - need(5) + 3 + 5);
    LBAR(5);
    TICK(5);
    {
      float sm = 0.f, sq = 0.f;
      const unsigned o2 = ones2();
      const lptr ha = lbase(L_HALO + lw * 5120 + lane16);
#pragma unroll
      for (int it = 0; it < 5; ++it) {
        const v4u v = lds128(ha, it * 1024);
        sm = dot2(v.x, o2, dot2(v.y, o2, dot2(v.z, o2, dot2(v.w, o2, sm))));
        sq = dot2(v.x, v.x, dot2(v.y, v.y, dot2(v.z, v.z, dot2(v.w, v.w, sq))));
      }
      sm += dppf<0xB1>(sm); sq += dppf<0xB1>(sq); sm += dppf<0x4E>(sm); sq += dppf<0x4E>(sq);
      sm += dppf<0x141>(sm); sq += dppf<0x141>(sq);
      const auto a = __builtin_amdgcn_permlane32_swap(asu(sm), asu(sm), false, false), q = __builtin_amdgcn_permlane32_swap(asu(sq), asu(sq), false, false);
      const float m = (asf(a[0]) + asf(a[1])) * (1.0f / D_), var = fmaxf((asf(q[0]) + asf(q[1])) * (1.0f / D_) - m * m, 0.f);
      stsf2(lbase(L_STAT + (4 * lw + ((lane >> 3) & 3)) * 8), 0, f2{m, __builtin_amdgcn_rsqf(var + EPS)});
    }
    WAIT_VM(ISS[5] - ISS[4]);
#pragma unroll
    for (int it = 0; it < 5; ++it) { const v2u lo = lds_tr(tv[it]), hi = lds_tr(tv[it] + 4 * prow); st128(v4u{lo.x, lo.y, hi.x, hi.y}, rw, dv[it], 0); }
    if (j3) {
#pragma unroll
      for (int it = 5; it < 9; ++it) { const v2u lo = lds_tr(tv[it]), hi = lds_tr(tv[it], 896); st128(v4u{lo.x, lo.y, hi.x, hi.y}, rw, dv[it], 0); }
    }
    static_assert(need(6) <= ISS[4] && need(7) <= ISS[4] && need(8) > ISS[5], "waits of ticks 5..7");
    WAIT_LGKM0();
    LBAR(6);
    TICK(6); LBAR(7);
    if (lw == 0 && lane < 3) {
      const int sg = j >> 2;
      flag_set(p.ws, lane < 2 ? F_H + (P * 2 + lane) * 256 + j * 8
                              : F_H + (((P + ((sg & 1) ? 15 : 1)) & 15) * 2 + (sg & 1)) * 256 + (16 + (j & 3) + 4 * (sg >> 1)) * 8, tag);
    }
    TICK(7); TOCK(7);
    if (lw == 1 && lane < 12)
      flag_set(p.ws, lane < 4 ? F_G1B + (j * 4 + lane) * 256 + P * 8 : F_G2 + (c * 8 + lane - 4) * 256 + (2 * P + half) * 8, tag);
    TICK(8); TOCK(8);
    TOCK(9);
    static_assert(ISS[10] <= 50, "no W2 unit in front of B11");
    if (lw == 0) flag_wait(p.ws, F_H + (P * 2 + half) * 256, 24, tag, lane);
    if (lw == 1) flag_wait2(p.ws, lane < 32 ? F_G2 + (c * 8 + (P >> 2) * 2 + half) * 256 + lane * 8 : F_G1B + (j * 4 + (P >> 2)) * 256 + (lane & 15) * 8, tag);
    STAMP(6);
    TOCK(10);
    TICK(11); TOCK(11);
    TICK(12); TOCK(12);
    TICK(13); TOCK(13);
    TICK(14); TOCK(14);
    TICK(15); TOCK(15);
    TICK(16); TOCK(16);
    TICK(17); TOCK(17);
    TICK(18); TOCK(18);
    TICK(19); LBAR(20);
    ring_issue<ISS[19], 100>(rw, lane16, sS1, sG2, sW3, lw1k);
    STAMP(7);
#pragma unroll
    for (int t = 0; t < 5; ++t) xs[t] = x1p[t] = v2u{0u, 0u};
  }

  v4u opiece, olast;
  {
    LANES();
    const int rl = 16 * wv + (lane >> 2), hd = (lane >> 1) & 1, hf_ = lane & 1;
    const lptr qa = lbase(L_QS + rl * 88 + hd * 40);
    const int own = (16 + rl - 8 * hf_ - 6) * 104, oth = (16 + rl - 8 * (1 - hf_) - 6) * 104;
    const lptr kA = lbase(L_KVB + hd * 40 + own), kB = lbase((hd ? L_KVC - 24 : L_KVB) + own);
    v2u q[5];
#pragma unroll
    for (int d = 0; d < 5; ++d) q[d] = lds64(qa, d * 8);
    float lg[4];
#pragma unroll
    for (int m = 0; m < 4; ++m) {
      float a = 0.f;
#pragma unroll
      for (int d = 0; d < 5; ++d) {
        const v2u k = lds64(d < 3 ? kA : kB, d * 8 + (3 - m) * 208);
        a = dot2(q[d].x, k.x, dot2(q[d].y, k.y, a));
      }
      lg[m] = (128 * P + rl - 2 * (4 * hf_ + m) >= 0) ? a * 0.22360679774997896f : -1e9f;
    }
    float mx = fmaxf(fmaxf(lg[0], lg[1]), fmaxf(lg[2], lg[3]));
    mx = fmaxf(mx, dppf<0xB1>(mx));
    float e[4], sum = 0.f;
#pragma unroll
    for (int m = 0; m < 4; ++m) { e[m] = __builtin_amdgcn_exp2f((lg[m] - mx) * 1.4426950408889634f); sum += e[m]; }
    sum += dppf<0xB1>(sum);
    const float inv = __builtin_amdgcn_rcpf(sum);
    unsigned pp[4];
    pp[0] = pack(e[0] * inv, e[1] * inv); pp[1] = pack(e[2] * inv, e[3] * inv);
    pp[2] = dpp<0xB1>(pp[0]); pp[3] = dpp<0xB1>(pp[1]);
    const int vo = L_KVC + 16 + hd * 40, vA = vo + (hf_ ? 24 : 0), vC = vo + (hf_ ? 20 : 16);
    const lptr vA0 = lbase(vA + own), vA1 = lbase(vA + oth), vC0 = lbase(vC + own), vC1 = lbase(vC + oth);
    float o[10] = {0.f, 0.f, 0.f, 0.f, 0.f, 0.f, 0.f, 0.f, 0.f, 0.f};
#pragma unroll
    for (int pr = 0; pr < 4; ++pr) {
      const lptr pa = pr < 2 ? vA0 : vA1, pc = pr < 2 ? vC0 : vC1;
      const int rb = (3 - 2 * (pr & 1)) * 208;
      const v2u a0 = lds64(pa, rb), b0 = lds64(pa, rb + 8), a1 = lds64(pa, rb - 208), b1 = lds64(pa, rb - 200);
      const unsigned c0 = lds32(pc, rb), c1 = lds32(pc, rb - 208);
      const unsigned u0[5] = {a0.x, a0.y, b0.x, b0.y, c0}, u1[5] = {a1.x, a1.y, b1.x, b1.y, c1};
#pragma unroll
      for (int d = 0; d < 5; ++d) {
        o[2 * d] = dot2(__builtin_amdgcn_perm(u1[d], u0[d], 0x05040100u), pp[pr], o[2 * d]);
        o[2 * d + 1] = dot2(__builtin_amdgcn_perm(u1[d], u0[d], 0x07060302u), pp[pr], o[2 * d + 1]);
      }
    }
    opiece = v4u{pack(o[0], o[1]), pack(o[2], o[3]), pack(o[4], o[5]), pack(o[6], o[7])};
    const unsigned l = pack(o[8], o[9]);
    olast = v4u{dpp<0x00>(l), dpp<0x55>(l), dpp<0xAA>(l), dpp<0xFF>(l)};
  }
  STAMP(11);
  WAIT_VM(0);
  WAIT_LGKM0();
  BAR();
  STAMP(12);
  {
    LANES();
    const int cc = 5 * j + (lane & 3), c4 = 5 * j + 4;
    const unsigned so = WS_O + (8 * P + wv) * RT_D;
    if (wv == 4 && lane < 2) flag_set(p.ws, F_X1 + (P * 2 + lane) * 256 + j * 8, tag);
    st128(opiece, rw, (cc >> 2) * 1024 + (cc & 3) * 256 + (lane >> 2) * 16, so);
    st128(olast, rw, (lane & 3) == 0 ? (unsigned)((lane >> 2) * 16) : OOB, so + (c4 >> 2) * 1024 + (c4 & 3) * 256);
  }

  if (wv < 4) {
    const int w = wv;
    LANES();
    {
      f4 cs[2] = {};
      const unsigned o1 = ones2();
      const v4u one = {o1, o1, o1, o1};
      colsum_step<20, 7>(cs, one, lane16, w); colsum_step<21, 7>(cs, one, lane16, w); colsum_step<22, 7>(cs, one, lane16, w);
      colsum_flush<7>(cs, lane, w);
    }
    WAIT_VM(0);
    BAR();
    STAMP(13);
    v4u of[20], w5[2][5];
    const Ring ring(lane16);
    {
      const unsigned so = WS_X1 + (8 * P + 2 * w) * RT_D, oso = WS_O + (8 * P + 4 * half + w) * RT_D;
      v4u af[2][20];
      auto lda = [&](int n) { af[n & 1][n >> 1] = ld128(rw, lane16 + ((n >> 1) & 3) * 1024, so + (n & 1) * RT_D + ((n >> 1) & ~3) * 1024); };
#pragma unroll
      for (int n = 0; n < 10; ++n) lda(n);
      SB0();
      f4 acc[2][7];
      v4u wf[7];
      v2u stp[2][2];
#pragma unroll
      for (int t = 0; t < 7; ++t) wf[t] = ring(300 + t);
      SB0();
#pragma unroll
      for (int kb = 0; kb < 20; ++kb) {
        if (kb && (kb & 3) == 0) BAR();
#pragma unroll
        for (int t = 0; t < 7; ++t) {
#pragma unroll
          for (int rt = 0; rt < 2; ++rt) mma(acc[rt][t], wf[t], af[rt][kb], kb == 0);
          SB0();
          if (kb + 1 < 20) wf[t] = ring(300 + 7 * (kb + 1) + t);
          if (t & 1) {
            const int n = 3 * kb + (t >> 1);
            if (kb < 10) lda(10 + n);
            else if (kb < 12 && t < 5)
              stp[kb - 10][t >> 1] = __builtin_amdgcn_raw_buffer_load_b64(rw, g * 256 + i16 * 8 + (t >> 1) * 128, WS_ST + (8 * P + 2 * w + kb - 10) * 1024, SC1);
            else if (kb >= 12 && n - 36 < 20) of[n - 36] = ld128(rw, lane16 + ((n - 36) & 3) * 1024, oso + ((n - 36) & ~3) * 1024);
          }
          SB0();
        }
      }
      ACC_DONE();
      BAR();
      STAMP(14);
      LANES();
      const lptr ca = lbase(L_CS + 16 * g);
      const Gelu gelu;
      float mm[2], rr[2];
#pragma unroll
      for (int rt = 0; rt < 2; ++rt) {
        const f4 s = __builtin_bit_cast(f4, v4u{stp[rt][0].x, stp[rt][0].y, stp[rt][1].x, stp[rt][1].y});
        const float m = sum_g(s[0] + s[2]) * 0.125f, d0 = s[0] - m, d1 = s[2] - m;
        const float m2 = sum_g(s[1] + s[3] + 80.f * (d0 * d0 + d1 * d1));
        mm[rt] = m; rr[rt] = __builtin_amdgcn_rsqf(m2 * (1.0f / D_) + EPS);
      }
      const unsigned kb0 = (s3 + 1) >> 1, kbl = (s3 & 1) ? s3 >> 1 : (s3 + 6) >> 1;
      const bool drop = (j & 3) == ((j >> 2) & 1);
#pragma unroll
      for (int tp = 0; tp < 3; ++tp) {
        const f4 c0 = colsum_get(ca, 2 * tp), c1 = colsum_get(ca, 2 * tp + 1);
#pragma unroll
        for (int rt = 0; rt < 2; ++rt) {
          const v2u a = pack4(gelu((acc[rt][2 * tp] - mm[rt] * c0) * rr[rt])), bb = pack4(gelu((acc[rt][2 * tp + 1] - mm[rt] * c1) * rr[rt]));
          st128(v4u{a.x, a.y, bb.x, bb.y}, rw, lane16, WS_H3 + (8 * P + 2 * w + rt) * RT_F + (kb0 + tp) * 1024);
        }
      }
      const f4 c6 = colsum_get(ca, 6);
#pragma unroll
      for (int t = 0; t < 5; ++t) w5[0][t] = ring(440 + t);
#pragma unroll
      for (int rt = 0; rt < 2; ++rt)
        st128(widen(pack4(gelu((acc[rt][6] - mm[rt] * c6) * rr[rt]))), rw, drop || (g & 1) ? OOB : (unsigned)((g >> 1) * 256 + i16 * 16),
              WS_H3 + (8 * P + 2 * w + rt) * RT_F + kbl * 1024 + (s3 & 1) * 512);
    }
    STAMP(15);
    f4 sacc[5];
    {
      SB0();
#pragma unroll
      for (int kb = 0; kb < 20; ++kb) {
        if (kb == 4) WAIT_VM(0);
        if (kb && (kb & 3) == 0) BAR();
#pragma unroll
        for (int t = 0; t < 5; ++t) {
          mma(sacc[t], w5[kb & 1][t], of[kb], kb == 0);
          SB0();
          if (kb + 1 < 20) w5[(kb + 1) & 1][t] = ring(440 + 5 * (kb + 1) + t);
          SB0();
        }
      }
      BAR();
    }
    STAMP(16);
    {
      v4u wf[2][5];
      BAR();
      STAMP(17);
      const unsigned so = WS_H3 + (8 * P + 4 * half + w) * RT_F;
      v4u af[54];
      auto lda = [&](int n) { af[n] = ld128(rw, lane16 + (n & 3) * 1024, so + (n & ~3) * 1024); };
#pragma unroll
      for (int n = 0; n < 10; ++n) lda(n);
      SB0();
      f4 acc[5];
#pragma unroll
      for (int t = 0; t < 5; ++t) wf[0][t] = ring(540 + t);
      SB0();
#pragma unroll
      for (int kb = 0; kb < 54; ++kb) {
        if (kb && (kb & 3) == 0) BAR();
#pragma unroll
        for (int t = 0; t < 5; ++t) {
          mma(acc[t], wf[kb & 1][t], af[kb], kb == 0);
          SB0();
          if (kb + 1 < 54) wf[(kb + 1) & 1][t] = ring(540 + 5 * (kb + 1) + t);
          if ((t & 1) && kb < 22) lda(10 + 2 * kb + (t >> 1));
          SB0();
        }
      }
      ACC_DONE();
      STAMP(18);
      LANES();
      v2u ov[5];
#pragma unroll
      for (int t = 0; t < 5; ++t) {
        const f4 bb = unpack4(x1p[t]) + acc[t];
        f4 gt;
#pragma unroll
        for (int e = 0; e < 4; ++e) gt[e] = __builtin_amdgcn_rcpf(1.0f + __builtin_amdgcn_exp2f(bb[e] * -1.4426950408889634f));
        ov[t] = pack4(unpack4(xs[t]) + sacc[t] * (bb * gt));
      }
      const rsrc_t ro = __builtin_amdgcn_make_buffer_rsrc(p.out, 0, S_ * D_ * 2, 0x00020000);
      const unsigned oo = (128 * P + 64 * half + 16 * w) * 1280 + 160 * c;
#pragma unroll
      for (int tp = 0; tp < 2; ++tp) {
        const auto sx = __builtin_amdgcn_permlane16_swap(ov[2 * tp].x, ov[2 * tp + 1].x, false, false);
        const auto sy = __builtin_amdgcn_permlane16_swap(ov[2 * tp].y, ov[2 * tp + 1].y, false, false);
        __builtin_amdgcn_raw_buffer_store_b128(v4u{sx[0], sy[0], sx[1], sy[1]}, ro,
                                               i16 * 1280 + (g & 1) * 32 + (g >> 1) * 16 + oo + 64 * tp + (c & 1) * 32, 0, 0);
      }
      __builtin_amdgcn_raw_buffer_store_b64(ov[4], ro, i16 * 1280 + g * 8, oo + ((c & 1) ? 0 : 128), 0);
    }
    STAMP(19);
    asm volatile("mk_code_to:");
  } else {
    const int lw = wv - 4;
    LANES();
    const unsigned lw1k = lw * 1024, sS1 = 0, sG2 = WS_G2 + c * G2_SL + lw1k, sW3 = WS_W3 + j * W3_SL + lw1k;
    f4 cs[2] = {};
    const unsigned o1 = ones2();
    const v4u one = {o1, o1, o1, o1};
    ring_issue<100, ISS[20]>(rw, lane16, sS1, sG2, sW3, lw1k);
    if (lw == 0) flag_wait(p.ws, F_X1 + (P * 2 + half) * 256, 16, tag, lane);
    STAMP(13); WAIT_VM(0); STAMP(14);
    BAR();
    if (lw == 0 && lane < 2) flag_set(p.ws, F_O + (P * 2 + lane) * 256 + j * 8, tag);
    __builtin_amdgcn_s_setprio(2);
    cs[0] = ldsf4(lbase(L_CS + lw * 64 + 16 * g)); cs[1] = ldsf4(lbase(L_CS + (lw < 3 ? lw + 4 : 6) * 64 + 16 * g));
    colsum_step<23, 7>(cs, one, lane16, lw); colsum_step<24, 7>(cs, one, lane16, lw); colsum_flush<7>(cs, lane, lw);
    __builtin_amdgcn_s_setprio(0);
    LBAR(21);
    LBAR(22);
    if (lw == 0) flag_wait(p.ws, F_O + (P * 2 + half) * 256, 16, tag, lane);
    TICK(22); TOCK(22);
    TICK(23); TOCK(23);
    TICK(24); TOCK(24);
    TICK(25); TOCK(25);
    if (lw == 0 && lane < 2) flag_set(p.ws, F_H3 + (P * 2 + lane) * 256 + j * 8, tag);
    TICK(26); TOCK(26);
    TICK(27); TOCK(27);
    TICK(28); TOCK(28);
    TICK(29); LBAR(30);
    TICK(30);
    if (lw == 0) flag_wait(p.ws, F_H3 + (P * 2 + half) * 256, 16, tag, lane);
    STAMP(15); WAIT_VM(ISS[30] - need(30)); STAMP(16);
    BAR();
    TOCK(30);
    TICK(31); TOCK(31); TICK(32); TOCK(32); TICK(33); TOCK(33); TICK(34); TOCK(34); TICK(35); TOCK(35); TICK(36); TOCK(36);
    TICK(37); TOCK(37); TICK(38); TOCK(38); TOCK(39); TOCK(40); TOCK(41); TOCK(42);
    if (b == 0 && lw == 1) {
#pragma unroll
      for (int q = 0; q < 4; ++q) flag_wait(p.ws, W_ARR + q * 512, 64, tag, lane);
      if (lane == 0) flag_set(p.ws, W_EPOCH, tag);
    }
  }
  STAMP_OUT();
}

}

extern "C" void kernel_launch(void* const* d_in, const int* in_sizes, int n_in, void* d_out, int out_size, void* d_ws, size_t ws_size,
                              hipStream_t stream) {
  Params p;
  p.x = (const u16*)d_in[0]; p.wqkv = (const u16*)d_in[1]; p.wo = (const u16*)d_in[2];
  p.w1 = (const u16*)d_in[4]; p.w2 = (const u16*)d_in[5]; p.w3 = (const u16*)d_in[6]; p.w4 = (const u16*)d_in[7];
  p.out = (u16*)d_out; p.ws = (char*)d_ws;
  (void)hipFuncSetAttribute((const void*)mk_kernel, hipFuncAttributeMaxDynamicSharedMemorySize, LDS_BYTES);
  mk_kernel<<<256, 512, LDS_BYTES, stream>>>(p);
}
```
